```python
import torch

def _softmax(x, axis=-1):
    return torch.softmax(x, dim=axis)
def _gelu(x):
    return torch.nn.functional.gelu(x, approximate="tanh")
def _silu(x):
    return torch.nn.functional.silu(x)
def _sigmoid(x):
    return torch.sigmoid(x)
def _layernorm(x, axis=-1, eps=1e-5):
    m = x.mean(dim=axis, keepdim=True)
    v = x.var(dim=axis, keepdim=True, unbiased=False)
    return (x - m) / torch.sqrt(v + eps)
def _scatter_add(tgt, idx, src, axis=0):
    return tgt.clone().index_add_(axis, idx, src)
def _segment_sum(src, seg, n):
    out = torch.zeros((n,) + src.shape[1:], dtype=src.dtype)
    return out.index_add_(0, seg, src)
def _pad(x, pw):
    flat = tuple(v for pair in reversed(pw) for v in pair)
    return torch.nn.functional.pad(x, flat)

kernel_name = 'concat_heads__mha_block_cached__gqa_block_cached__near_ridge'

def setup_inputs():

    _w = (lambda a: (lambda t: t.to(torch.bfloat16) if t.is_floating_point() else t)(torch.as_tensor(a)))
    def _A_setup():
        B, Sq, D = 8, 144, 512
        H, Sk, Dh = 32, 1024, 128
        return {
            "x": _w((lambda *s: torch.randn(*s, dtype=torch.bfloat16) * 0.8)(B, Sq, D)),
            "k_cache": _w((lambda *s: torch.randn(*s, dtype=torch.bfloat16) * 0.8)(B, H, Sk, Dh)),
            "v_cache": _w((lambda *s: torch.randn(*s, dtype=torch.bfloat16) * 0.05)(B, H, Sk, Dh)),
            "wq": _w((lambda *s: torch.randn(*s, dtype=torch.bfloat16) * 0.8)(D, H * Dh) * (D ** -0.5)),
            "wo": _w((lambda *s: torch.randn(*s, dtype=torch.bfloat16) * 0.05)(H * Dh, D) * ((H * Dh) ** -0.5)),
        }
    _A_in = _A_setup()

    def _B_setup():
        B, Sq, D = 8, 144, 512
        Hq, Hkv, Sk, Dh = 32, 32, 1024, 128
        assert Hq % Hkv == 0, (Hq, Hkv)
        return {
            "x": _w((lambda *s: torch.randn(*s, dtype=torch.bfloat16) * 0.8)(B, Sq, D)),
            "k_cache": _w((lambda *s: torch.randn(*s, dtype=torch.bfloat16) * 0.8)(B, Hkv, Sk, Dh)),
            "v_cache": _w((lambda *s: torch.randn(*s, dtype=torch.bfloat16) * 0.05)(B, Hkv, Sk, Dh)),
            "wq": _w((lambda *s: torch.randn(*s, dtype=torch.bfloat16) * 0.8)(D, Hq * Dh) * (D ** -0.5)),
            "wo": _w((lambda *s: torch.randn(*s, dtype=torch.bfloat16) * 0.05)(Hq * Dh, D) * ((Hq * Dh) ** -0.5)),
        }
    _B_in = _B_setup()

    return {
        "A_x": _A_in["x"],
        "A_k_cache": _A_in["k_cache"],
        "A_v_cache": _A_in["v_cache"],
        "A_wq": _A_in["wq"],
        "A_wo": _A_in["wo"],
        "B_x": _B_in["x"],
        "B_k_cache": _B_in["k_cache"],
        "B_v_cache": _B_in["v_cache"],
        "B_wq": _B_in["wq"],
        "B_wo": _B_in["wo"],
    }

def reference(A_x, A_k_cache, A_v_cache, A_wq, A_wo, B_x, B_k_cache, B_v_cache, B_wq, B_wo):
    def _A_ref(x, k_cache, v_cache, wq, wo):
        B, Sq, D = x.shape
        H, Dh = 32, 128
        q = (lambda x, *d: x.permute(*d))((x @ wq).reshape(B, Sq, H, Dh), 0, 2, 1, 3)
        scores = torch.einsum("bhqd,bhkd->bhqk", q, k_cache) * (Dh ** -0.5)
        p = _softmax(scores, axis=-1)
        o = torch.einsum("bhqk,bhkd->bhqd", p, v_cache)
        return (lambda x, *d: x.permute(*d))(o, 0, 2, 1, 3).reshape(B, Sq, H * Dh) @ wo

    def _B_ref(x, k_cache, v_cache, wq, wo):
        B, Sq, D = x.shape
        Hq, Dh = 32, 128
        Hkv = k_cache.shape[1]
        rep = Hq // Hkv
        q = (lambda x, *d: x.permute(*d))((x @ wq).reshape(B, Sq, Hq, Dh), 0, 2, 1, 3)
        k = (lambda x, n, axis: torch.repeat_interleave(x, n, dim=axis))(k_cache, rep, axis=1)
        v = (lambda x, n, axis: torch.repeat_interleave(x, n, dim=axis))(v_cache, rep, axis=1)
        scores = torch.einsum("bhqd,bhkd->bhqk", q, k) * (Dh ** -0.5)
        p = _softmax(scores, axis=-1)
        o = torch.einsum("bhqk,bhkd->bhqd", p, v)
        return (lambda x, *d: x.permute(*d))(o, 0, 2, 1, 3).reshape(B, Sq, Hq * Dh) @ wo

    a = _A_ref(A_x, A_k_cache, A_v_cache, A_wq, A_wo)
    b = _B_ref(B_x, B_k_cache, B_v_cache, B_wq, B_wo)
    return torch.concatenate([a, b], axis=-1)
```

```cpp
#include <hip/hip_runtime.h>
#include <stdint.h>

#define DI __device__ __forceinline__
#define LAS __attribute__((address_space(3)))
typedef __bf16 bf16x2 __attribute__((ext_vector_type(2)));
typedef __bf16 bf16x4 __attribute__((ext_vector_type(4)));
typedef __bf16 bf16x8 __attribute__((ext_vector_type(8)));
typedef float f32x2 __attribute__((ext_vector_type(2)));
typedef float f32x4 __attribute__((ext_vector_type(4)));
typedef unsigned u32x2 __attribute__((ext_vector_type(2)));
typedef unsigned u32x4 __attribute__((ext_vector_type(4)));
typedef __amdgpu_buffer_rsrc_t rsrc_t;
typedef unsigned lptr;
typedef unsigned long long u64;
typedef __attribute__((address_space(1))) u64 gu64;

constexpr int SLOT_KV = 16384, SLOT_G = 17408;
constexpr int LDS_K = 0, LDS_V = 3 * SLOT_KV, LDS_G = 6 * SLOT_KV, LDS_MISC = LDS_G + 3 * SLOT_G;
constexpr int LDS_B4 = LDS_G - SLOT_G;
DI constexpr unsigned blk_slot(int i) { return (unsigned)(((i + 1) & 3) * SLOT_G); }
constexpr int LDS_NUL = LDS_MISC + 12288;
constexpr int LDS_EPOCH = LDS_NUL + 256;
constexpr int LDS_TOTAL = 163840;
static_assert(LDS_NUL + 256 <= LDS_TOTAL - 384, "LDS map");
constexpr int HEAD_BYTES = 1024 * 256;
constexpr int OFRAG_HEAD = 4 * 9 * 1024;
constexpr int SLAB_BYTES = 36 * 1024;
constexpr size_t WS_FLAG_O = 4096, WS_FLAG_S = WS_FLAG_O + 2 * 64 * 128, WS_O = 65536, WS_SLAB = WS_O + (size_t)512 * OFRAG_HEAD;
constexpr int SHIFT = 896;
constexpr float QSCALE = 0.08838834764831845f * 1.4426950408889634f;
constexpr float PSUM_LIMIT = 4096.f;
DI constexpr int qx_region(int third) { return third == 0 ? LDS_G + SLOT_G : (third == 1 ? LDS_G + 2 * SLOT_G : LDS_MISC); }

#define KV_POL "nt"
#define KVB_POL KV_POL
#define W_POL ""
#define X_POL ""
#define O_POL "sc1"
#define PF_D 4

DI rsrc_t mk_rsrc(const void* p, unsigned bytes) { return __builtin_amdgcn_make_buffer_rsrc((void*)p, (short)0, (int)bytes, 0x00020000); }
#define DMA(POL, OFF, VOFF, SRD, SOFF) \
  asm volatile("buffer_load_dwordx4 %0, %1, %2 offen offset:" #OFF " " POL " lds" ::"v"(VOFF), "s"(SRD), "s"(SOFF) : "memory")
#define DMA16(POL, VOFF, SRD, SOFF) \
  asm volatile("s_mov_b64 exec, 0xffff\n\tbuffer_load_dwordx4 %0, %1, %2 offen " POL " lds\n\ts_mov_b64 exec, -1" ::"v"(VOFF), "s"(SRD), "s"(SOFF) : "memory")
#define WARM_POL ""
#define WARM(VOFF, SRD) asm volatile("buffer_load_dword %0, %1, 0 offen " WARM_POL " lds" ::"v"(VOFF), "s"(SRD) : "memory")
DI void set_m0(unsigned v) { asm volatile("s_mov_b32 m0, %0\n\ts_nop 0" ::"s"(v) : "memory"); }
template <unsigned OFF> DI void set_m0_at(unsigned base) { asm volatile("s_add_u32 m0, %0, %1\n\ts_nop 0" ::"s"(base), "i"(OFF) : "memory", "scc"); }
template <int N> DI void wait_bar() { asm volatile("s_waitcnt vmcnt(%0) lgkmcnt(0)\n\ts_barrier" ::"i"(N) : "memory"); }
DI void lds_bar() { asm volatile("s_waitcnt lgkmcnt(0)\n\ts_barrier" ::: "memory"); }
#define SB() __builtin_amdgcn_sched_barrier(0)
template <class T> DI void pin(T& x) { asm volatile("" : "+v"(x)); }
#define JSTAMP(i) {}
#define OSTAMP(i) {}
#define ISTAMP(i) {}
#define STAMP(x)
#define RSTAMP(x)
#define LSTAMP(i) {}
#define LRSTAMP(i) {}

DI f32x4 mfma(bf16x8 a, bf16x8 b, f32x4 c) { return __builtin_amdgcn_mfma_f32_16x16x32_bf16(a, b, c, 0, 0, 0); }
DI void mfma_ip(f32x4& c, bf16x8 a, bf16x8 b) { asm volatile("v_mfma_f32_16x16x32_bf16 %0, %1, %2, %0" : "+v"(c) : "v"(a), "v"(b)); }
DI void mfma_c(f32x4& d, bf16x8 a, bf16x8 b, f32x4 c) { asm volatile("v_mfma_f32_16x16x32_bf16 %0, %1, %2, %3" : "=&v"(d) : "v"(a), "v"(b), "v"(c)); }
DI void mfma_0(f32x4& d, bf16x8 a, bf16x8 b) { asm volatile("v_mfma_f32_16x16x32_bf16 %0, %1, %2, 0" : "=&v"(d) : "v"(a), "v"(b)); }
DI void settle8(f32x4 (&t)[8]) {
  asm volatile("s_nop 7\n\ts_nop 1" : "+v"(t[0]), "+v"(t[1]), "+v"(t[2]), "+v"(t[3]), "+v"(t[4]), "+v"(t[5]), "+v"(t[6]), "+v"(t[7]));
}
DI void settle4(f32x4 (&t)[4]) { asm volatile("s_nop 7\n\ts_nop 1" : "+v"(t[0]), "+v"(t[1]), "+v"(t[2]), "+v"(t[3])); }
DI void settle1(f32x4& t) { asm volatile("s_nop 7\n\ts_nop 1" : "+v"(t)); }
template <class T> DI LAS T* lds_ptr(lptr p) { return (LAS T*)(size_t)p; }
DI bf16x8 ld_b128(lptr p) { return *lds_ptr<bf16x8>(p); }
DI bf16x8 ld_tr2(lptr p0, lptr p1) {
  bf16x4 a = __builtin_amdgcn_ds_read_tr16_b64_v4bf16(lds_ptr<bf16x4>(p0));
  bf16x4 b = __builtin_amdgcn_ds_read_tr16_b64_v4bf16(lds_ptr<bf16x4>(p1));
  return __builtin_shufflevector(a, b, 0, 1, 2, 3, 4, 5, 6, 7);
}
DI unsigned cvtpk(float lo, float hi) { f32x2 v = {lo, hi}; return __builtin_bit_cast(unsigned, __builtin_convertvector(v, bf16x2)); }
DI bf16x8 pack8(f32x4 a, f32x4 b) {
  u32x4 w = {cvtpk(a[0], a[1]), cvtpk(a[2], a[3]), cvtpk(b[0], b[1]), cvtpk(b[2], b[3])};
  return __builtin_bit_cast(bf16x8, w);
}
DI bf16x8 pack_frag(f32x4 t0, f32x4 t1) {
  unsigned a0 = cvtpk(t0[0], t0[1]), a1 = cvtpk(t0[2], t0[3]), b0 = cvtpk(t1[0], t1[1]), b1 = cvtpk(t1[2], t1[3]);
  auto r0 = __builtin_amdgcn_permlane32_swap(a0, b0, false, false);
  auto r1 = __builtin_amdgcn_permlane32_swap(a1, b1, false, false);
  auto q0 = __builtin_amdgcn_permlane16_swap(r0[0], r0[1], false, false);
  auto q1 = __builtin_amdgcn_permlane16_swap(r1[0], r1[1], false, false);
  u32x4 w = {q0[0], q1[0], q0[1], q1[1]};
  return __builtin_bit_cast(bf16x8, w);
}
DI float xmax(float x) {
  auto a = __builtin_amdgcn_permlane32_swap(__float_as_uint(x), __float_as_uint(x), false, false);
  x = fmaxf(__uint_as_float(a[0]), __uint_as_float(a[1]));
  auto b = __builtin_amdgcn_permlane16_swap(__float_as_uint(x), __float_as_uint(x), false, false);
  return fmaxf(__uint_as_float(b[0]), __uint_as_float(b[1]));
}
DI float xsum(float x) {
  auto a = __builtin_amdgcn_permlane32_swap(__float_as_uint(x), __float_as_uint(x), false, false);
  x = __uint_as_float(a[0]) + __uint_as_float(a[1]);
  auto b = __builtin_amdgcn_permlane16_swap(__float_as_uint(x), __float_as_uint(x), false, false);
  return __uint_as_float(b[0]) + __uint_as_float(b[1]);
}
DI f32x4 splat(float x) { return f32x4{x, x, x, x}; }
DI float ex2(float x) { return __builtin_amdgcn_exp2f(x); }
DI float max4(f32x4 v) { return fmaxf(fmaxf(v[0], v[1]), fmaxf(v[2], v[3])); }
DI f32x4 exp4(f32x4 v) { return f32x4{ex2(v[0]), ex2(v[1]), ex2(v[2]), ex2(v[3])}; }
DI float sum4(f32x4 v) { return (v[0] + v[1]) + (v[2] + v[3]); }
DI void flag_store(char* p, u64 tag) { __hip_atomic_store((gu64*)p, tag, __ATOMIC_RELAXED, __HIP_MEMORY_SCOPE_AGENT); }
DI u64 flag_load(const char* p) { return __hip_atomic_load((gu64*)p, __ATOMIC_RELAXED, __HIP_MEMORY_SCOPE_AGENT); }
DI void flag_finish(const char* p, u64 v, u64 tag) {
  for (unsigned spins = 0; !__all(v == tag) && spins < (1u << 22); ++spins) { __builtin_amdgcn_s_sleep(2); v = flag_load(p); }
  asm volatile("" ::: "memory");
}
#define ST_AUX 16
DI void store_wt(rsrc_t r, unsigned voff, unsigned off, u32x4 v) { __builtin_amdgcn_raw_buffer_store_b128(v, r, voff + off, 0, ST_AUX); }
DI int lane_id() { int l; asm volatile("v_mbcnt_lo_u32_b32 %0, -1, 0\n\tv_mbcnt_hi_u32_b32 %0, -1, %0" : "=v"(l)); return l; }

DI unsigned k_addr(int l, int u) { const int m = l & 15, g = l >> 4; return 2048 * (m >> 2) + 64 * (4 * u + (m & 3)) + 16 * (g ^ ((2 * (m >> 2) + u) & 3)); }
DI unsigned tr_addr(int l, int d, int t, int gstride) {
  const int g = l >> 4, q = (l & 15) >> 2, p = l & 3;
  return gstride * g + 64 * (4 * t + q) + 16 * ((2 * d + (p >> 1)) ^ ((2 * g + t) & 3)) + 8 * (p & 1);
}
DI unsigned x_addr(int l, bool xfrag) { return xfrag ? 16 * l : 64 * (l & 15) + 16 * ((l >> 4) ^ (3 * ((l & 15) >> 3))); }
DI constexpr unsigned x_tile(int qt) { return qt < 8 ? 4096 * (qt >> 1) + 2048 + 1024 * (qt & 1) : 16384; }

struct Ctx { lptr lds; int w; };
DI u64 epoch_tag(const Ctx& c) {
  const u32x2 v = *lds_ptr<u32x2>(c.lds + LDS_EPOCH);
  return (((u64)(unsigned)__builtin_amdgcn_readfirstlane(v[1]) << 32) | (unsigned)__builtin_amdgcn_readfirstlane(v[0])) + 1;
}
struct Ldr { unsigned kv[4], wv[2], xv[2], x9; };
DI Ldr make_ldr(int j, unsigned strideW, bool xfrag) {
  Ldr r;
  const int l = lane_id(), row = (l >> 2) & 7, x = (l >> 4) & 1, s = l & 3;
  const unsigned oe = row * 256 + 64 * (l >> 5) + 16 * (s ^ x), oo = row * 256 + 64 * (l >> 5) + 16 * (s ^ (2 + x));
  r.kv[0] = oe + SHIFT; r.kv[1] = oe; r.kv[2] = oo + SHIFT; r.kv[3] = oo;
  const unsigned ow = row * strideW + 64 * (l >> 5) + 16 * (s ^ ((2 * j + x) & 3));
  r.wv[0] = ow + SHIFT; r.wv[1] = ow;
  if (xfrag) {
    r.xv[0] = r.xv[1] = 16 * l;
    r.x9 = 2048 + 8192 + 16 * l;
  } else {
    r.xv[0] = (l >> 2) * 1024 + 16 * (s ^ (3 * (l >> 5)));
    r.xv[1] = r.xv[0] + 16384 - 1024;
    r.x9 = 2048 + (128 + (l >> 2)) * 1024 + 16 * (s ^ (3 * (j >> 1)));
  }
  return r;
}
struct Issue { rsrc_t rk, rv, rx, rw; unsigned mk, mv, mg, m9, sk, sv, sx, sw, s9; };
DI void issue_piece(const Ldr& L, const Issue& is, int k) {
  if (k == 0) { set_m0(is.mk); DMA(KV_POL, 0, L.kv[0], is.rk, is.sk); }
  if (k == 1) DMA(KV_POL, 1024, L.kv[1], is.rk, is.sk);
  if (k == 2) DMA(KV_POL, 2048, L.kv[2], is.rk, is.sk);
  if (k == 3) DMA(KV_POL, 3072, L.kv[3], is.rk, is.sk);
  if (k == 4) { set_m0(is.mv); DMA(KV_POL, 0, L.kv[0], is.rv, is.sv); }
  if (k == 5) DMA(KV_POL, 1024, L.kv[1], is.rv, is.sv);
  if (k == 6) DMA(KV_POL, 2048, L.kv[2], is.rv, is.sv);
  if (k == 7) DMA(KV_POL, 3072, L.kv[3], is.rv, is.sv);
  if (k == 8) { set_m0(is.mg); DMA(W_POL, 0, L.wv[0], is.rw, is.sw); }
  if (k == 9) DMA(W_POL, 1024, L.wv[1], is.rw, is.sw);
  if (k == 10) DMA(X_POL, 2048, L.xv[0], is.rx, is.sx);
  if (k == 11) DMA(X_POL, 3072, L.xv[1], is.rx, is.sx);
  if (k == 12) { set_m0(is.m9); DMA16(X_POL, L.x9, is.rx, is.s9); }
}
DI void issue_piece_b(const Ldr& L, const Issue& is, int k) {
  if (k == 0) { set_m0(is.mk); DMA(KVB_POL, 0, L.kv[0], is.rk, is.sk); }
  if (k == 1) DMA(KVB_POL, 1024, L.kv[1], is.rk, is.sk);
  if (k == 2) DMA(KVB_POL, 2048, L.kv[2], is.rk, is.sk);
  if (k == 3) DMA(KVB_POL, 3072, L.kv[3], is.rk, is.sk);
  if (k == 4) { set_m0(is.mv); DMA(KVB_POL, 0, L.kv[0], is.rv, is.sv); }
  if (k == 5) DMA(KVB_POL, 1024, L.kv[1], is.rv, is.sv);
  if (k == 6) DMA(KVB_POL, 2048, L.kv[2], is.rv, is.sv);
  if (k == 7) DMA(KVB_POL, 3072, L.kv[3], is.rv, is.sv);
}
DI void issue_range_b(const Ldr& L, const Issue& is, int k0, int k1) {
#pragma unroll
  for (int k = k0; k < k1; ++k) issue_piece_b(L, is, k);
}
DI void issue_piece_o(const Ldr& L, const Issue& is, int k) {
  if (k == 8) { set_m0(is.mg); DMA(W_POL, 0, L.wv[0], is.rw, is.sw); }
  if (k == 9) DMA(W_POL, 1024, L.wv[1], is.rw, is.sw);
  if (k == 10) DMA(O_POL, 2048, L.xv[0], is.rx, is.sx);
  if (k == 11) DMA(O_POL, 3072, L.xv[1], is.rx, is.sx);
  if (k == 12) { set_m0(is.m9); DMA16(O_POL, L.x9, is.rx, is.s9); }
}
DI void issue_range_o(const Ldr& L, const Issue& is, int k0, int k1) {
#pragma unroll
  for (int k = k0; k < k1; ++k) issue_piece_o(L, is, k);
}
DI void issue_range(const Ldr& L, const Issue& is, int k0, int k1) {
#pragma unroll
  for (int k = k0; k < k1; ++k) issue_piece(L, is, k);
}

template <int NQ> struct Att {
  f32x4 O[NQ][8];
  bf16x8 Q[NQ][4];
  f32x4 negm[NQ];
  float l[NQ];
  bf16x8 P[NQ][2];
};
struct AddrA { unsigned k[2], t[2][2]; };
DI void shift_addr(AddrA& a, unsigned dk, unsigned dv) {
  a.k[0] += dk; a.k[1] += dk; a.t[0][0] += dv; a.t[0][1] += dv; a.t[1][0] += dv; a.t[1][1] += dv;
}
DI bf16x8 k_frag(const AddrA& ad, int i) { return ld_b128(ad.k[(i >> 2) & 1] + 8192 * (i >> 3) + 512 * (i & 3)); }
DI bf16x8 v_frag(const AddrA& ad, int j) {
  const int o = 8192 * (j >> 3) + 512 * ((j & 7) >> 1);
  return ld_tr2(ad.t[j & 1][0] + o, ad.t[j & 1][1] + o);
}
DI bf16x8 ones_frag() { u32x4 o = {0x3f803f80u, 0x3f803f80u, 0x3f803f80u, 0x3f803f80u}; pin(o); return __builtin_bit_cast(bf16x8, o); }
DI float max16(const f32x4 (&S)[4]) { return fmaxf(fmaxf(max4(S[0]), max4(S[1])), fmaxf(max4(S[2]), max4(S[3]))); }
DI float probs(const f32x4 (&S)[4], float d, bf16x8 (&P)[2]) {
  f32x4 p[4];
#pragma unroll
  for (int i = 0; i < 4; ++i) p[i] = exp4(S[i] - d);
  P[0] = pack8(p[0], p[1]); P[1] = pack8(p[2], p[3]);
  return xsum((sum4(p[0]) + sum4(p[1])) + (sum4(p[2]) + sum4(p[3])));
}
constexpr int NF = 6;

template <int NQ> DI void qk_first(const Att<NQ>& a, f32x4 (&S)[NQ][4], const AddrA& ad) {
  bf16x8 f[NF];
#pragma unroll
  for (int i = 0; i < 4; ++i) f[i ^ 1] = k_frag(ad, i ^ 1);
  SB();
#pragma unroll
  for (int i = 0; i < 16; ++i) {
    if (!(i & 1) && i + 4 < 16) { f[(i + 5) % NF] = k_frag(ad, i + 5); f[(i + 4) % NF] = k_frag(ad, i + 4); }
#pragma unroll
    for (int q = 0; q < NQ; ++q) {
      if ((i & 3) == 0) mfma_0(S[q][i >> 2], f[i % NF], a.Q[q][0]); else mfma_ip(S[q][i >> 2], f[i % NF], a.Q[q][i & 3]);
    }
    SB();
  }
#pragma unroll
  for (int q = 0; q < NQ; ++q) settle4(S[q]);
}
template <int NQ> DI void pv_last(Att<NQ>& a, const AddrA& ad) {
  bf16x8 f[NF];
#pragma unroll
  for (int i = 0; i < 4; ++i) f[i ^ 1] = v_frag(ad, i ^ 1);
  SB();
#pragma unroll
  for (int j = 0; j < 16; ++j) {
    if (!(j & 1) && j + 4 < 16) { f[(j + 5) % NF] = v_frag(ad, j + 5); f[(j + 4) % NF] = v_frag(ad, j + 4); }
#pragma unroll
    for (int q = 0; q < NQ; ++q) mfma_ip(a.O[q][j & 7], f[j % NF], a.P[q][j >> 3]);
    SB();
  }
#pragma unroll
  for (int q = 0; q < NQ; ++q) settle8(a.O[q]);
}

DI void step_two(Att<2>& a, const AddrA& ad, bf16x8 ones) {
  bf16x8 f[NF];
  f32x4 S[2][4], ts[2];
  u32x4 Pn[2][2];
  float e0[2];
  auto load = [&](int i) { return i < 16 ? k_frag(ad, i) : v_frag(ad, i - 16); };
#pragma unroll
  for (int i = 0; i < 4; ++i) f[i ^ 1] = load(i ^ 1);
  SB();
#pragma unroll
  for (int i = 0; i < 32; ++i) {
    if (!(i & 1) && i + 4 < 32) { f[(i + 5) % NF] = load(i + 5); f[(i + 4) % NF] = load(i + 4); }
    const bf16x8 fr = f[i % NF];
#pragma unroll
    for (int q = 0; q < 2; ++q) {
      if (i < 16) {
        if ((i & 3) == 0) mfma_c(S[q][i >> 2], fr, a.Q[q][0], a.negm[q]); else mfma_ip(S[q][i >> 2], fr, a.Q[q][i & 3]);
      } else {
        const int j = i - 16;
        mfma_ip(a.O[q][j & 7], fr, a.P[q][j >> 3]);
        const float e = ex2(S[q][j >> 2][j & 3]);
        if (j & 1) { unsigned pw = cvtpk(e0[q], e); pin(pw); Pn[q][j >> 3][(j & 7) >> 1] = pw; } else { e0[q] = e; pin(e0[q]); }
        if (j == 9) mfma_0(ts[q], ones, __builtin_bit_cast(bf16x8, Pn[q][0]));
      }
    }
    SB();
  }
  asm volatile("s_nop 1");
#pragma unroll
  for (int q = 0; q < 2; ++q) mfma_ip(ts[q], ones, __builtin_bit_cast(bf16x8, Pn[q][1]));
  asm volatile("s_nop 7\n\ts_nop 1" : "+v"(ts[0]), "+v"(ts[1]));
  const bool bad = !(ts[0][0] <= PSUM_LIMIT) || !(ts[1][0] <= PSUM_LIMIT);
  if (__builtin_expect(__any(bad), 0)) {
#pragma unroll
    for (int q = 0; q < 2; ++q) {
      f32x4 R[4];
#pragma unroll
      for (int kt = 0; kt < 4; ++kt) {
        f32x4 sa = a.negm[q];
#pragma unroll
        for (int st = 0; st < 4; ++st) sa = mfma(k_frag(ad, 4 * kt + st), a.Q[q][st], sa);
        R[kt] = sa;
        SB();
      }
      const float d = fmaxf(xmax(max16(R)), 0.f), f = ex2(-d);
      a.negm[q] = a.negm[q] - d;
      a.l[q] *= f;
#pragma unroll
      for (int i = 0; i < 8; ++i) a.O[q][i] = a.O[q][i] * f;
      bf16x8 P[2];
      ts[q][0] = probs(R, d, P);
      Pn[q][0] = __builtin_bit_cast(u32x4, P[0]); Pn[q][1] = __builtin_bit_cast(u32x4, P[1]);
      SB();
    }
  }
#pragma unroll
  for (int q = 0; q < 2; ++q) {
    a.l[q] += ts[q][0]; a.P[q][0] = __builtin_bit_cast(bf16x8, Pn[q][0]); a.P[q][1] = __builtin_bit_cast(bf16x8, Pn[q][1]);
  }
}

template <bool QK>
DI void step_one(Att<1>& a, f32x4 (&So)[4], const AddrA& ad, bf16x8 ones) {
  bf16x8 f[NF];
  f32x4 S[4], ts;
  u32x4 Pn[2];
  float e0;
  if (QK) {
#pragma unroll
    for (int i = 0; i < 4; ++i) f[i ^ 1] = k_frag(ad, i ^ 1);
  }
  SB();
#pragma unroll
  for (int i = 0; i < 16; ++i) {
    if (QK) {
      if (!(i & 1) && i + 4 < 16) { f[(i + 5) % NF] = k_frag(ad, i + 5); f[(i + 4) % NF] = k_frag(ad, i + 4); }
      if ((i & 3) == 0) mfma_c(S[i >> 2], f[i % NF], a.Q[0][0], a.negm[0]); else mfma_ip(S[i >> 2], f[i % NF], a.Q[0][i & 3]);
    }
    const float e = ex2(So[i >> 2][i & 3]);
    if (i & 1) { unsigned pw = cvtpk(e0, e); pin(pw); Pn[i >> 3][(i & 7) >> 1] = pw; } else { e0 = e; pin(e0); }
    if (i == 9) mfma_0(ts, ones, __builtin_bit_cast(bf16x8, Pn[0]));
    SB();
  }
#pragma unroll
  for (int i = 0; i < 4; ++i) f[i ^ 1] = v_frag(ad, i ^ 1);
  mfma_ip(ts, ones, __builtin_bit_cast(bf16x8, Pn[1]));
  settle1(ts);
  if (__builtin_expect(__any(!(ts[0] <= PSUM_LIMIT)), 0)) {
    const float d = fmaxf(xmax(max16(So)), 0.f), f2 = ex2(-d);
    a.negm[0] = a.negm[0] - d;
    a.l[0] *= f2;
#pragma unroll
    for (int i = 0; i < 8; ++i) a.O[0][i] = a.O[0][i] * f2;
    if (QK) {
#pragma unroll
      for (int i = 0; i < 4; ++i) S[i] = S[i] - d;
    }
    bf16x8 P[2];
    ts[0] = probs(So, d, P);
    Pn[0] = __builtin_bit_cast(u32x4, P[0]); Pn[1] = __builtin_bit_cast(u32x4, P[1]);
    SB();
    asm volatile("s_nop 1");
  }
  a.l[0] += ts[0];
  SB();
#pragma unroll
  for (int j = 0; j < 16; ++j) {
    if (!(j & 1) && j + 4 < 16) { f[(j + 5) % NF] = v_frag(ad, j + 5); f[(j + 4) % NF] = v_frag(ad, j + 4); }
    mfma_ip(a.O[0][j & 7], f[j % NF], __builtin_bit_cast(bf16x8, Pn[j >> 3]));
    SB();
  }
  if (QK) {
#pragma unroll
    for (int i = 0; i < 4; ++i) So[i] = S[i];
  }
}
template <int NQ> DI void fetch_q(const Ctx& c, Att<NQ>& a, int qt0) {
  const unsigned lo = c.lds + lane_id() * 16;
#pragma unroll
  for (int q = 0; q < NQ; ++q) {
    const int qt = qt0 + q, th = qt < 3 ? 0 : (qt < 6 ? 1 : 2);
    const unsigned base = lo + (th == 0 ? qx_region(0) : (th == 1 ? qx_region(1) : qx_region(2))) + (qt - 3 * th) * 4096;
#pragma unroll
    for (int s = 0; s < 4; ++s) a.Q[q][s] = ld_b128(base + s * 1024);
  }
}
DI void store_frags(const f32x4 (&O)[8], float inv, rsrc_t dst, unsigned off, bf16x8 (&f)[4]) {
  const unsigned lo = lane_id() * 16;
#pragma unroll
  for (int s = 0; s < 4; ++s) { f[s] = pack_frag(O[2 * s] * inv, O[2 * s + 1] * inv); store_wt(dst, lo, off + s * 9216, __builtin_bit_cast(u32x4, f[s])); }
}
DI void keep_frags(const Ctx& c, int b, unsigned tile, const bf16x8 (&f)[4]) {
  const unsigned lo = c.lds + tile + lane_id() * 16;
#pragma unroll
  for (int s = 0; s < 4; ++s) *lds_ptr<bf16x8>(lo + ((s - b + 1) & 3) * SLOT_G) = f[s];
}

struct Streams { rsrc_t kA, vA, kB, vB, xA, wA, xB, wB, nul; };
struct Job { int e, b, hh; char* ws; u64 tag; };
DI char* flag_o(const Job& j, int br) { return j.ws + WS_FLAG_O + (size_t)((br * 8 + j.e) * 8 + j.b) * 128; }
DI char* flag_s(const Job& j, int br) { return j.ws + WS_FLAG_S + (size_t)((br * 8 + j.b) * 4 + j.hh) * 128; }
DI char* slab_group(const Job& j, int br) { return j.ws + WS_SLAB + (size_t)((br * 8 + j.b) * 4 + j.hh) * 8 * SLAB_BYTES; }
DI rsrc_t o_group(const Job& j, int br, int shift) {
  return mk_rsrc(j.ws + WS_O + (size_t)((br * 8 + j.b) * 32 + 4 * j.e) * OFRAG_HEAD - shift, 4 * OFRAG_HEAD + shift);
}
DI int next_slot(int s) { return s == 2 ? 0 : s + 1; }
DI AddrA att_addr(const Ctx& c) {
  AddrA ad;
  const int l = lane_id();
  ad.k[0] = c.lds + LDS_K + k_addr(l, 0); ad.k[1] = c.lds + LDS_K + k_addr(l, 1);
#pragma unroll
  for (int d = 0; d < 2; ++d)
#pragma unroll
    for (int t = 0; t < 2; ++t) ad.t[d][t] = c.lds + LDS_V + 2 * SLOT_KV + tr_addr(l, d, t, 2048);
  return ad;
}
DI void att_advance(AddrA& ad, int& s0) {
  shift_addr(ad, s0 == 2 ? -2 * SLOT_KV : SLOT_KV, s0 == 0 ? -2 * SLOT_KV : SLOT_KV);
  s0 = next_slot(s0);
}

DI void two_tile_wave(const Ctx& c, const Job& jb, const bf16x8 (&Q0)[2][4]) {
  Att<2> a;
  AddrA ad = att_addr(c);
  const bf16x8 ones = ones_frag();
  int s0 = 0;
#pragma unroll
  for (int q = 0; q < 2; ++q)
#pragma unroll
    for (int s = 0; s < 4; ++s) a.Q[q][s] = Q0[q][s];
#pragma clang loop unroll(disable)
  for (int r = 0; r < 2; ++r) {
    {
      f32x4 S[2][4];
      qk_first<2>(a, S, ad);
#pragma unroll
      for (int q = 0; q < 2; ++q) {
        const float m = xmax(max16(S[q]));
        a.negm[q] = splat(-m);
        a.l[q] = probs(S[q], m, a.P[q]);
#pragma unroll
        for (int i = 0; i < 8; ++i) { a.O[q][i] = splat(0.f); pin(a.O[q][i]); }
        SB();
      }
    }
    LSTAMP(r ? 6 : 2)
#pragma clang loop unroll(disable)
    for (int t = 1; t < 16; ++t) {
      att_advance(ad, s0);
      wait_bar<0>();
      step_two(a, ad, ones);
    }
    LSTAMP(r ? 7 : 3)
    att_advance(ad, s0);
    wait_bar<0>();
    pv_last<2>(a, ad);
    LSTAMP(r ? 8 : 4)
    bf16x8 f[2][4];
#pragma unroll
    for (int q = 0; q < 2; ++q) store_frags(a.O[q], 1.f / a.l[q], o_group(jb, r, 0), jb.hh * OFRAG_HEAD + (2 * c.w + q) * 1024, f[q]);
    lds_bar();
    if (r == 0) {
      fetch_q<2>(c, a, 2 * c.w);
      LSTAMP(5)
    } else {
#pragma unroll
      for (int q = 0; q < 2; ++q) keep_frags(c, jb.b, 4096 * c.w + 2048 + 1024 * q, f[q]);
    }
  }
}

DI void ninth_tile_wave(const Ctx& c, const Job& jb) {
  Att<1> a;
  AddrA ad = att_addr(c);
  const bf16x8 ones = ones_frag();
  int s0 = 0;
  fetch_q<1>(c, a, 8);
#pragma clang loop unroll(disable)
  for (int r = 0; r < 2; ++r) {
    f32x4 So[4];
    {
      f32x4 S[1][4];
      qk_first<1>(a, S, ad);
      const float m = xmax(max16(S[0]));
      a.negm[0] = splat(-m);
      a.l[0] = 0.f;
#pragma unroll
      for (int i = 0; i < 4; ++i) So[i] = S[0][i] - m;
#pragma unroll
      for (int i = 0; i < 8; ++i) { a.O[0][i] = splat(0.f); pin(a.O[0][i]); }
    }
    LSTAMP(r ? 6 : 2)
    u64 seen = 0;
    const char* group = flag_o(jb, 0) + 8 * (lane_id() & 3);
#pragma clang loop unroll(disable)
    for (int t = 1; t < 16; ++t) {
      att_advance(ad, s0);
      wait_bar<0>();
      if (r == 1) {
        if (t == 1 && lane_id() == 0) flag_store(flag_o(jb, 0) + 8 * jb.hh, jb.tag);
        if (t == 2) seen = flag_load(group);
        if (t == 4) flag_finish(group, seen, jb.tag);
      }
      step_one<true>(a, So, ad, ones);
    }
    LSTAMP(r ? 7 : 3)
    att_advance(ad, s0);
    wait_bar<0>();
    step_one<false>(a, So, ad, ones);
    settle8(a.O[0]);
    LSTAMP(r ? 8 : 4)
    bf16x8 f[4];
    store_frags(a.O[0], 1.f / a.l[0], o_group(jb, r, 0), jb.hh * OFRAG_HEAD + 8 * 1024, f);
    lds_bar();
    if (r == 0) {
      fetch_q<1>(c, a, 8);
      LSTAMP(5)
    } else {
      keep_frags(c, jb.b, 16384, f);
    }
  }
}

DI rsrc_t wo_slice(const char* wo, const Job& jb) {
  return mk_rsrc(wo + (size_t)jb.e * 512 * 1024 + jb.hh * 256 - SHIFT, 512 * 1024 - jb.hh * 256 + SHIFT);
}
DI int tail_chunk_index(const Job& jb, int i) { return 4 * ((jb.hh + (i >> 2)) & 3) + ((i + jb.b) & 3); }
DI void tail_weights(const Ctx& c, const Job& jb, const Ldr& T, const char* woB, int i0, int i1);

template <int N1, int N2, int N3> DI void wait_bar_j(int J) {
  if (J == 1) wait_bar<N1>(); else if (J == 2) wait_bar<N2>(); else wait_bar<N3>();
}
DI void wait_kv(int J) { wait_bar_j<12, 8, 12>(J); }
DI void wait_all(int J) { wait_bar_j<18, 17, 17>(J); }
DI void wait_g(int J) { wait_bar_j<6, 9, 5>(J); }
template <bool SECOND> DI void issue_kv(const Ldr& L, const Issue& is, int J, bool with_k) {
  if (with_k) {
    if (SECOND) issue_range_b(L, is, 0, 4); else issue_range(L, is, 0, 4);
    if (J == 1) { Issue t = is; t.mk -= 4096; t.sk -= 4096; if (SECOND) issue_range_b(L, t, 0, 4); else issue_range(L, t, 0, 4); }
  }
  if (SECOND) issue_range_b(L, is, 4, 8); else issue_range(L, is, 4, 8);
  if (J == 3) { Issue t = is; t.mv -= 3 * 4096; t.sv -= 3 * 4096; if (SECOND) issue_range_b(L, t, 4, 8); else issue_range(L, t, 4, 8); }
}
template <bool HANDED> DI void issue_g(const Ldr& L, const Issue& is, int J, unsigned dw, unsigned dx, unsigned d9) {
  if (HANDED) issue_range_o(L, is, 8, 13); else issue_range(L, is, 8, 13);
  if (J == 1) { Issue t = is; t.m9 -= 256; t.s9 -= d9; if (HANDED) issue_range_o(L, t, 12, 13); else issue_range(L, t, 12, 13); }
  if (J == 2) { Issue t = is; t.mg -= 2 * 4096; t.sw -= 2 * dw; t.sx -= 2 * dx; if (HANDED) issue_range_o(L, t, 8, 12); else issue_range(L, t, 8, 12); }
}
DI void projection_wave(const Ctx& c, const Streams& st, int c0, const Job& jb, const char* woA, const char* woB, f32x4 (&acc)[3][8]) {
  const int J = c.w - 4, gi = J - 1;
  const Ldr L = make_ldr(J, 8192, false);
  unsigned wt[2][2], xa[3];
  int gs;
  auto restart = [&](bool xfrag) {
    const int l = lane_id();
#pragma unroll
    for (int q = 0; q < 3; ++q)
#pragma unroll
      for (int n = 0; n < 8; ++n) { acc[q][n] = splat(0.f); pin(acc[q][n]); }
#pragma unroll
    for (int d = 0; d < 2; ++d)
#pragma unroll
      for (int t = 0; t < 2; ++t) wt[d][t] = c.lds + LDS_G + tr_addr(l, d, t, 4096);
#pragma unroll
    for (int q = 0; q < 3; ++q) {
      const int qt = 3 * gi + q;
      xa[q] = c.lds + LDS_G + x_addr(l, xfrag) + (qt < 8 ? 4096 * (qt >> 1) + 2048 + 1024 * (qt & 1) : 16384);
    }
    gs = 0;
  };
  restart(false);
  const unsigned kend = c.lds + LDS_K + 3 * SLOT_KV + 4096 * J, vend = c.lds + LDS_V + 3 * SLOT_KV + 4096 * J, gend = c.lds + LDS_G + 3 * SLOT_G;
  struct { unsigned mk, mv, mg, sk, sv; rsrc_t rk, rv; } rg;
  int ch = (c0 + 1) & 15;
  auto mk_issue = [&](rsrc_t rx, rsrc_t rw) {
    Issue is;
    is.rk = rg.rk; is.rv = rg.rv; is.rx = rx; is.rw = rw;
    is.mk = rg.mk; is.mv = rg.mv; is.mg = rg.mg + 4096 * J; is.m9 = rg.mg + 16384 + 256 * J;
    is.sk = rg.sk + 4096 * J; is.sv = rg.sv + 4096 * J;
    is.sw = ch * (32 * 8192) + J * (8 * 8192); is.sx = ch * 64 + J * (32 * 1024); is.s9 = ch * 64 + J * 4096;
    return is;
  };
  auto next = [&]() {
    rg.mk = rg.mk + SLOT_KV == kend ? rg.mk - 2 * SLOT_KV : rg.mk + SLOT_KV;
    rg.mv = rg.mv + SLOT_KV == vend ? rg.mv - 2 * SLOT_KV : rg.mv + SLOT_KV;
    rg.mg = rg.mg + SLOT_G == gend ? rg.mg - 2 * SLOT_G : rg.mg + SLOT_G;
    rg.sk += 16384; rg.sv += 16384; ch = (ch + 1) & 15;
  };
  rg.mk = c.lds + LDS_K + 2 * SLOT_KV + 4096 * J; rg.mv = c.lds + LDS_V + SLOT_KV + 4096 * J; rg.mg = c.lds + LDS_G + SLOT_G;
  rg.sk = 2 * 16384; rg.sv = 16384; rg.rk = st.kA; rg.rv = st.vA;
  issue_g<false>(L, mk_issue(st.xB, st.wB), J, 8 * 8192, 32 * 1024, 4096);
  next();
  if (J == 1) asm volatile("s_waitcnt vmcnt(14)" ::: "memory"); else if (J == 2) asm volatile("s_waitcnt vmcnt(17)" ::: "memory"); else asm volatile("s_waitcnt vmcnt(13)" ::: "memory");
  auto gemm = [&]() {
    bf16x8 x[3], f[8];
#pragma unroll
    for (int q = 0; q < 3; ++q) x[q] = ld_b128(xa[q]);
#pragma unroll
    for (int n = 0; n < 4; ++n) f[n ^ 1] = ld_tr2(wt[(n ^ 1) & 1][0] + 512 * (n >> 1), wt[(n ^ 1) & 1][1] + 512 * (n >> 1));
    SB();
#pragma unroll
    for (int n = 0; n < 8; ++n) {
      if (!(n & 1) && n < 4) {
        f[n + 5] = ld_tr2(wt[1][0] + 512 * ((n + 4) >> 1), wt[1][1] + 512 * ((n + 4) >> 1));
        f[n + 4] = ld_tr2(wt[0][0] + 512 * ((n + 4) >> 1), wt[0][1] + 512 * ((n + 4) >> 1));
      }
#pragma unroll
      for (int q = 0; q < 3; ++q) mfma_ip(acc[q][n], f[n], x[q]);
      SB();
    }
    const unsigned dv = gs == 2 ? -2 * SLOT_G : SLOT_G;
#pragma unroll
    for (int d = 0; d < 2; ++d) { wt[d][0] += dv; wt[d][1] += dv; }
#pragma unroll
    for (int q = 0; q < 3; ++q) xa[q] += dv;
    gs = next_slot(gs);
  };
#pragma clang loop unroll(disable)
  for (int t = 1; t < 17; ++t) {
    if (t == 14) { rg.rk = st.kB; rg.sk = 0; }
    if (t == 15) { rg.rv = st.vB; rg.sv = 0; }
    if (t < 16) wait_all(J); else wait_kv(J);
    const Issue is = mk_issue(st.xB, st.wB);
    issue_kv<false>(L, is, J, true);
    if (t < 15) issue_g<false>(L, is, J, 8 * 8192, 32 * 1024, 4096);
    gemm();
    next();
  }
  {
#pragma unroll
    for (int q = 0; q < 3; ++q) settle8(acc[q]);
    const unsigned base = c.lds + lane_id() * 16 + (gi == 0 ? qx_region(0) : (gi == 1 ? qx_region(1) : qx_region(2)));
#pragma unroll
    for (int q = 0; q < 3; ++q)
#pragma unroll
      for (int s = 0; s < 4; ++s) *lds_ptr<bf16x8>(base + (q * 4 + s) * 1024) = pack_frag(acc[q][2 * s] * QSCALE, acc[q][2 * s + 1] * QSCALE);
    lds_bar();
  }
  restart(true);
  const Ldr T = make_ldr(J, 1024, true);
  const rsrc_t rxA = o_group(jb, 0, 2048), rwA = wo_slice(woA, jb);
  const rsrc_t warmB = mk_rsrc(woB + (size_t)jb.e * 512 * 1024 + (jb.b * 4 + jb.hh) * 16384, 16384);
  const unsigned wl = lane_id() * 16;
#pragma clang loop unroll(disable)
  for (int t = 1; t < 22; ++t) {
    if (t == 1) wait_kv(J);
    else if (t <= 14) wait_all(J);
    else if (t == 15) wait_bar_j<10, 13, 13>(J);
    else if (t <= 17) wait_g(J);
    else if (t == 18) wait_bar<6>();
    else wait_bar<4>();
    if (J == 2 && t >= 6 && t < 12) {
      set_m0(c.lds + LDS_MISC);
      const unsigned so = (t - 6) * 3072;
      DMA(W_POL, 0, wl, warmB, so); DMA(W_POL, 1024, wl, warmB, so); DMA(W_POL, 2048, wl, warmB, so);
    }
    Issue is = mk_issue(rxA, rwA);
    if (t <= 14) issue_kv<true>(L, is, J, t <= 13);
    if (t <= 16) {
      const int cc = tail_chunk_index(jb, t - 1);
      is.sw = cc * (32 * 1024) + J * (8 * 1024); is.sx = cc * 9216 + J * 2048; is.s9 = cc * 9216 + J * 256;
      issue_g<true>(T, is, J, 8 * 1024, 2048, 256);
    }
    if (t == 17) tail_weights(c, jb, T, woB, 0, 4);
    if (t >= 19) tail_weights(c, jb, T, woB, t - 15, t - 14);
    if ((t >= 3 && t <= 17) || t == 21) gemm();
    next();
  }
#pragma unroll
  for (int q = 0; q < 3; ++q) settle8(acc[q]);
}

struct Blk { f32x4 a[2][8], n[2]; };
struct BlkFrag { bf16x8 w[8], x[2], w9[2], x9; };
struct BlkAddr { unsigned t[2][2], t9[2][2], x, x9; };
DI BlkAddr blk_addr(const Ctx& c, bool xfrag, unsigned ring) {
  BlkAddr a;
  const int l = lane_id();
  const unsigned base = c.lds + ring;
#pragma unroll
  for (int d = 0; d < 2; ++d)
#pragma unroll
    for (int t = 0; t < 2; ++t) { a.t[d][t] = base + tr_addr(l, d, t, 4096); a.t9[d][t] = a.t[d][t] + 512 * c.w; }
  a.x = base + x_addr(l, xfrag) + 4096 * c.w + 2048;
  a.x9 = base + x_addr(l, xfrag) + 16384;
  return a;
}
DI void blk_read(BlkFrag& f, const BlkAddr& a, unsigned o, int k) {
  if (k == 0) { f.x[0] = ld_b128(a.x + o); f.x[1] = ld_b128(a.x + o + 1024); }
  if (k >= 1 && k <= 8) f.w[k - 1] = ld_tr2(a.t[(k - 1) & 1][0] + o + 512 * ((k - 1) >> 1), a.t[(k - 1) & 1][1] + o + 512 * ((k - 1) >> 1));
  if (k == 9) { f.x9 = ld_b128(a.x9 + o); f.w9[0] = ld_tr2(a.t9[0][0] + o, a.t9[0][1] + o); }
  if (k == 10) f.w9[1] = ld_tr2(a.t9[1][0] + o, a.t9[1][1] + o);
}
DI void blk_load(BlkFrag& f, const BlkAddr& a, unsigned o) {
#pragma unroll
  for (int k = 0; k < 11; ++k) blk_read(f, a, o, k);
}
DI void blk_zero(Blk& acc) {
#pragma unroll
  for (int n = 0; n < 8; ++n) { acc.a[0][n] = splat(0.f); acc.a[1][n] = splat(0.f); pin(acc.a[0][n]); pin(acc.a[1][n]); }
  acc.n[0] = splat(0.f); acc.n[1] = splat(0.f); pin(acc.n[0]); pin(acc.n[1]);
}
DI void blk_settle(Blk& acc) { settle8(acc.a[0]); settle8(acc.a[1]); settle1(acc.n[0]); settle1(acc.n[1]); }
template <bool MORE, unsigned RD> DI void blk_step(Blk& acc, const BlkFrag& f, BlkFrag& nx, const BlkAddr& a) {
#pragma unroll
  for (int i = 0; i < 18; ++i) {
    if (i < 16) mfma_ip(acc.a[i & 1][i >> 1], f.w[i >> 1], f.x[i & 1]); else mfma_ip(acc.n[i - 16], f.w9[i - 16], f.x9);
    if (MORE && i < 11) blk_read(nx, a, RD, i);
    SB();
  }
}
struct ChunkSrc { rsrc_t rx, rw; unsigned sw, sx, s9; };
template <unsigned WR, bool HANDED = false> DI void blk_issue(const Ldr& L, const ChunkSrc& s, unsigned mj, unsigned m9) {
  set_m0_at<WR>(mj);
  DMA(W_POL, 0, L.wv[0], s.rw, s.sw);
  DMA(W_POL, 1024, L.wv[1], s.rw, s.sw);
  if (HANDED) { DMA(O_POL, 2048, L.xv[0], s.rx, s.sx); DMA(O_POL, 3072, L.xv[1], s.rx, s.sx); }
  else { DMA(X_POL, 2048, L.xv[0], s.rx, s.sx); DMA(X_POL, 3072, L.xv[1], s.rx, s.sx); }
  set_m0_at<WR>(m9);
  if (HANDED) DMA16(O_POL, L.x9, s.rx, s.s9); else DMA16(X_POL, L.x9, s.rx, s.s9);
}

DI void first_projection_compute(const Ctx& c, Blk& acc) {
  blk_zero(acc);
  const BlkAddr ba = blk_addr(c, false, LDS_B4);
  BlkFrag f0, f1;
  lds_bar();
  blk_load(f0, ba, blk_slot(0));
#define INIT_STEP(U, FC, FN) { lds_bar(); blk_step<true, blk_slot((U) + 1)>(acc, FC, FN, ba); }
#pragma clang loop unroll(disable)
  for (int ch = 0; ch < 16; ch += 4) { INIT_STEP(0, f0, f1) INIT_STEP(1, f1, f0) INIT_STEP(2, f0, f1) INIT_STEP(3, f1, f0) }
#undef INIT_STEP
  blk_settle(acc);
}
DI void first_projection_load(const Ctx& c, const Streams& st, int c0) {
  const int j = c.w & 3;
  const Ldr L = make_ldr(j, 8192, false);
  const unsigned mj = c.lds + LDS_B4 + 4096 * j, m9 = c.lds + LDS_B4 + 16384 + 256 * j;
  auto src = [&](rsrc_t rx, rsrc_t rw, int ch) {
    ChunkSrc s;
    const int cc = (c0 + ch) & 15;
    s.rx = rx; s.rw = rw; s.sw = cc * (32 * 8192) + j * 8 * 8192; s.sx = cc * 64 + j * 32 * 1024; s.s9 = cc * 64 + j * 4096;
    return s;
  };
  auto kv_issue = [&](rsrc_t rkv, int tile, unsigned base) {
    Issue is;
    is.rk = rkv; is.rv = is.rx = is.rw = st.nul; is.mk = c.lds + base + 4096 * j; is.sk = tile * 16384 + 4096 * j;
    is.mv = is.mg = is.m9 = is.sv = is.sx = is.sw = is.s9 = 0;
    issue_range(L, is, 0, 4);
  };
  blk_issue<blk_slot(0)>(L, src(st.xA, st.wA, 0), mj, m9);
  blk_issue<blk_slot(1)>(L, src(st.xA, st.wA, 1), mj, m9);
  blk_issue<blk_slot(2)>(L, src(st.xA, st.wA, 2), mj, m9);
  kv_issue(st.kA, 0, LDS_K);
  wait_bar<14>();
  wait_bar<9>();  blk_issue<blk_slot(3)>(L, src(st.xA, st.wA, 3), mj, m9); kv_issue(st.kA, 1, LDS_K + SLOT_KV);
  wait_bar<13>(); blk_issue<blk_slot(0)>(L, src(st.xA, st.wA, 4), mj, m9);
  wait_bar<9>();  blk_issue<blk_slot(1)>(L, src(st.xA, st.wA, 5), mj, m9);
  wait_bar<5>();  blk_issue<blk_slot(2)>(L, src(st.xA, st.wA, 6), mj, m9);
#pragma clang loop unroll(disable)
  for (int ch = 4; ch < 12; ch += 4) {
    wait_bar<5>(); blk_issue<blk_slot(3)>(L, src(st.xA, st.wA, ch + 3), mj, m9);
    wait_bar<5>(); blk_issue<blk_slot(0)>(L, src(st.xA, st.wA, ch + 4), mj, m9);
    wait_bar<5>(); blk_issue<blk_slot(1)>(L, src(st.xA, st.wA, ch + 5), mj, m9);
    wait_bar<5>(); blk_issue<blk_slot(2)>(L, src(st.xA, st.wA, ch + 6), mj, m9);
  }
  wait_bar<5>(); blk_issue<blk_slot(3)>(L, src(st.xA, st.wA, 15), mj, m9);
  wait_bar<5>(); kv_issue(st.vA, 0, LDS_V); blk_issue<blk_slot(0)>(L, src(st.xB, st.wB, 0), mj, m9);
  wait_bar<9>(); kv_issue(st.kA, 2, LDS_K + 2 * SLOT_KV);
  wait_bar<13>(); kv_issue(st.vA, 1, LDS_V + SLOT_KV);
}

constexpr int LDS_T4 = 0;
DI ChunkSrc tail_src(const Job& jb, rsrc_t rx, rsrc_t rw, int i, int j) {
  ChunkSrc s;
  const int cc = tail_chunk_index(jb, i);
  s.rx = rx; s.rw = rw; s.sw = cc * 32 * 1024 + j * 8 * 1024; s.sx = cc * 9216 + j * 2048; s.s9 = cc * 9216 + j * 256;
  return s;
}
DI void tail_weights(const Ctx& c, const Job& jb, const Ldr& T, const char* woB, int i0, int i1) {
  const int j = c.w & 3;
  const rsrc_t rw = wo_slice(woB, jb);
#pragma clang loop unroll(disable)
  for (int i = i0; i < i1; ++i) {
    set_m0(c.lds + LDS_T4 + 4096 * j + ((i + 1) & 3) * SLOT_G);
    const unsigned sw = tail_chunk_index(jb, i) * (32 * 1024) + j * (8 * 1024);
    DMA(W_POL, 0, T.wv[0], rw, sw);
    DMA(W_POL, 1024, T.wv[1], rw, sw);
  }
}
DI void out_projection_compute(const Ctx& c, const Job& jb) {
  const int l = lane_id();
  const BlkAddr ba = blk_addr(c, true, LDS_T4);
  BlkFrag f0, f1;
  Blk acc;
  blk_zero(acc);
  const char* pollO = flag_o(jb, 1) + 8 * (l & 3);
  u64 seen = 0;
  lds_bar();
  blk_load(f0, ba, blk_slot(0));
#define TAIL_STEP(CH, U, FC, FN)                                                       \
  {                                                                                    \
    if ((U) == 0 && (CH) == 0) {                                                       \
      wait_bar<0>();                                                                   \
      if (c.w == 0 && l == 0) flag_store(flag_o(jb, 1) + 8 * jb.hh, jb.tag);           \
      if (c.w == 1) seen = flag_load(pollO);                                           \
    } else {                                                                           \
      lds_bar();                                                                       \
    }                                                                                  \
    OSTAMP(CH)                                                                         \
    if ((U) == 0 && (CH) == 4 && c.w == 0 && l == 0) flag_store(flag_s(jb, 0) + 8 * jb.e, jb.tag); \
    SB();                                                                              \
    blk_step<true, blk_slot((U) + 1)>(acc, FC, FN, ba);                                \
    if ((U) == 2 && (CH) == 2) {                                                       \
      if (c.w == 1) flag_finish(pollO, seen, jb.tag);                                  \
      lds_bar();                                                                       \
    }                                                                                  \
  }
#pragma clang loop unroll(disable)
  for (int ch = 0; ch < 16; ch += 4) { TAIL_STEP(ch, 0, f0, f1) TAIL_STEP(ch + 1, 1, f1, f0) TAIL_STEP(ch + 2, 2, f0, f1) TAIL_STEP(ch + 3, 3, f1, f0) }
#undef TAIL_STEP
  blk_settle(acc);
  const rsrc_t slab = mk_rsrc(slab_group(jb, 1) + jb.e * SLAB_BYTES, SLAB_BYTES);
#pragma unroll
  for (int q = 0; q < 2; ++q)
#pragma unroll
    for (int s = 0; s < 4; ++s)
      store_wt(slab, l * 16, ((2 * c.w + q) * 4 + s) * 1024, __builtin_bit_cast(u32x4, pack_frag(acc.a[q][2 * s], acc.a[q][2 * s + 1])));
  store_wt(slab, l * 16, (32 + c.w) * 1024, __builtin_bit_cast(u32x4, pack_frag(acc.n[0], acc.n[1])));
}
DI void out_projection_load(const Ctx& c, const Job& jb, const char* woB, const f32x4 (&accA)[3][8]) {
  const int j = c.w & 3;
  const Ldr L = make_ldr(j, 1024, true);
  const unsigned mj = c.lds + LDS_T4 + 4096 * j, m9 = c.lds + LDS_T4 + 16384 + 256 * j;
  const rsrc_t rx = o_group(jb, 1, 2048), rw = wo_slice(woB, jb);
  if (j) {
    const rsrc_t slab = mk_rsrc(slab_group(jb, 0) + jb.e * SLAB_BYTES, SLAB_BYTES);
    const unsigned lo = lane_id() * 16 + 3 * (j - 1) * 4096;
#pragma unroll
    for (int q = 0; q < 3; ++q)
#pragma unroll
      for (int s = 0; s < 4; ++s) store_wt(slab, lo, (q * 4 + s) * 1024, __builtin_bit_cast(u32x4, pack_frag(accA[q][2 * s], accA[q][2 * s + 1])));
  }
  unsigned sx[3];
#pragma unroll
  for (int i = 0; i < 3; ++i) { sx[i] = tail_chunk_index(jb, 4 + i) * 9216; asm volatile("" : "+s"(sx[i])); }
  lds_bar();
#pragma unroll
  for (int i = 0; i < 3; ++i) {
    const unsigned slot = ((5 + i) & 3) * SLOT_G;
    set_m0(mj + slot);
    DMA(O_POL, 2048, L.xv[0], rx, sx[i] + j * 2048);
    DMA(O_POL, 3072, L.xv[1], rx, sx[i] + j * 2048);
    set_m0(m9 + slot);
    DMA16(O_POL, L.x9, rx, sx[i] + j * 256);
  }
  wait_bar<6>();  blk_issue<blk_slot(7), true>(L, tail_src(jb, rx, rw, 7, j), mj, m9);
  wait_bar<8>();  blk_issue<blk_slot(8), true>(L, tail_src(jb, rx, rw, 8, j), mj, m9);
  wait_bar<10>(); blk_issue<blk_slot(9), true>(L, tail_src(jb, rx, rw, 9, j), mj, m9);
  wait_bar<10>(); blk_issue<blk_slot(10), true>(L, tail_src(jb, rx, rw, 10, j), mj, m9);
  wait_bar<10>(); blk_issue<blk_slot(11), true>(L, tail_src(jb, rx, rw, 11, j), mj, m9);
  wait_bar<10>(); blk_issue<blk_slot(12), true>(L, tail_src(jb, rx, rw, 12, j), mj, m9);
  wait_bar<10>(); blk_issue<blk_slot(13), true>(L, tail_src(jb, rx, rw, 13, j), mj, m9);
  wait_bar<10>(); blk_issue<blk_slot(14), true>(L, tail_src(jb, rx, rw, 14, j), mj, m9);
  wait_bar<10>(); blk_issue<blk_slot(15), true>(L, tail_src(jb, rx, rw, 15, j), mj, m9);
  wait_bar<10>();
  wait_bar<5>();
  wait_bar<0>();
  wait_bar<0>();
}
struct Piece { u32x4 t[8]; };
DI void piece_load(Piece& pc, const Job& jb, int br, int qt, int s) {
  const unsigned lo = lane_id() * 16;
  const rsrc_t r = mk_rsrc(slab_group(jb, br), 8 * SLAB_BYTES);
#pragma unroll
  for (int e = 0; e < 8; ++e) pc.t[e] = __builtin_amdgcn_raw_buffer_load_b128(r, lo, e * SLAB_BYTES + (qt * 4 + s) * 1024, 16);
}
DI void piece_store(const Piece& pc, char* out, const Job& jb, int br, int qt, int s) {
  float t[8];
#pragma unroll
  for (int k = 0; k < 8; ++k) t[k] = 0.f;
#pragma unroll
  for (int e = 0; e < 8; ++e)
#pragma unroll
    for (int k = 0; k < 4; ++k) { t[2 * k] += __uint_as_float(pc.t[e][k] << 16); t[2 * k + 1] += __uint_as_float(pc.t[e][k] & 0xffff0000u); }
  const int l = lane_id();
  const u32x4 w = {cvtpk(t[0], t[1]), cvtpk(t[2], t[3]), cvtpk(t[4], t[5]), cvtpk(t[6], t[7])};
  __builtin_amdgcn_raw_buffer_store_b128(w, mk_rsrc(out + (size_t)jb.b * 144 * 2048, 144 * 2048), (qt * 16 + (l & 15)) * 2048 + br * 1024 + jb.hh * 256 + s * 64 + (l >> 4) * 16, 0, 16);
}
DI void reduce_out(const Ctx& c, const Job& jb, char* out) {
  const int s = jb.e & 3, hf = jb.e >> 2, l = lane_id();
  const int nA = hf ? 4 : 5, qA = hf ? 5 : 0, nB = 9 - nA, qB = hf ? 0 : 5;
  Piece pc;
  if (c.w == 7) { const char* poll = flag_s(jb, 0) + 8 * (l & 7); flag_finish(poll, flag_load(poll), jb.tag); }
  asm volatile("s_waitcnt vmcnt(0)" ::: "memory");
  lds_bar();
  LSTAMP(12)
  if (c.w == 7 && l == 0) flag_store(flag_s(jb, 1) + 8 * jb.e, jb.tag);
  if (c.w < nA) { piece_load(pc, jb, 0, qA + c.w, s); piece_store(pc, out, jb, 0, qA + c.w, s); }
  if (c.w == 7) { const char* poll = flag_s(jb, 1) + 8 * (l & 7); flag_finish(poll, flag_load(poll), jb.tag); }
  lds_bar();
  LSTAMP(13)
  if (c.w < nB) { piece_load(pc, jb, 1, qB + c.w, s); piece_store(pc, out, jb, 1, qB + c.w, s); }
  if (blockIdx.x == 0 && c.w == 7) {
#pragma unroll
    for (int k = 0; k < 4; ++k) {
      const char* p = jb.ws + WS_FLAG_O + l * 128 + 8 * k;
      u64 v = flag_load(p);
      for (unsigned spins = 0; !__all(v == jb.tag) && spins < (1u << 22); ++spins) { __builtin_amdgcn_s_sleep(2); v = flag_load(p); }
    }
    if (l == 0) flag_store(jb.ws, jb.tag);
  }
}

__global__ __launch_bounds__(512, 2) void attn_kernel(const char* __restrict__ xA, const char* __restrict__ kA, const char* __restrict__ vA,
                                                       const char* __restrict__ wqA, const char* __restrict__ woA, const char* __restrict__ xB,
                                                       const char* __restrict__ kB, const char* __restrict__ vB, const char* __restrict__ wqB,
                                                       const char* __restrict__ woB, char* __restrict__ out, char* __restrict__ ws) {
  extern __shared__ __attribute__((aligned(16))) char lds_raw[];
  Ctx c;
  c.lds = (unsigned)(size_t)(LAS char*)lds_raw;
  c.w = __builtin_amdgcn_readfirstlane(threadIdx.x >> 6);
  const int e = blockIdx.x & 7, r = blockIdx.x >> 3, b = r >> 2, h = 4 * e + (r & 3);
  Streams st;
  const size_t hoff = (size_t)(b * 32 + h) * HEAD_BYTES;
  st.kA = mk_rsrc(kA + hoff - SHIFT, HEAD_BYTES + SHIFT); st.vA = mk_rsrc(vA + hoff - SHIFT, HEAD_BYTES + SHIFT);
  st.kB = mk_rsrc(kB + hoff - SHIFT, HEAD_BYTES + SHIFT); st.vB = mk_rsrc(vB + hoff - SHIFT, HEAD_BYTES + SHIFT);
  st.xA = mk_rsrc(xA + (size_t)b * 144 * 1024 - 2048, 144 * 1024 + 2048); st.xB = mk_rsrc(xB + (size_t)b * 144 * 1024 - 2048, 144 * 1024 + 2048);
  st.wA = mk_rsrc(wqA + h * 256 - SHIFT, 512 * 8192 - h * 256 + SHIFT); st.wB = mk_rsrc(wqB + h * 256 - SHIFT, 512 * 8192 - h * 256 + SHIFT);
  st.nul = mk_rsrc(kA, 0);
  Job jb;
  jb.e = e; jb.b = b; jb.hh = r & 3; jb.ws = ws;
  LRSTAMP(14) LSTAMP(0)

  if (c.w == 4) {
    set_m0(c.lds + LDS_EPOCH);
    asm volatile("s_mov_b64 exec, 1\n\tbuffer_load_dwordx4 %0, %1, 0 offen sc1 lds\n\ts_mov_b64 exec, -1" ::"v"(0), "s"(mk_rsrc(ws, 16)) : "memory");
  }
  if (c.w == 0) {
    const int l = lane_id();
    const char* p = kB + hoff;
    if (l == 1) p = kB + hoff + HEAD_BYTES - 4;
    if (l == 2) p = vB + hoff;
    if (l == 3) p = vB + hoff + HEAD_BYTES - 4;
    if (l == 4) p = vA + hoff;
    if (l == 5) p = xB + (size_t)b * 144 * 1024;
    if (l == 6) p = xB + (size_t)b * 144 * 1024 + 144 * 1024 - 4;
    if (l == 7) p = wqB + h * 256;
    if (l == 8) p = wqB + h * 256 + 256 * 8192;
    if (l == 9) p = wqB + h * 256 + 511 * 8192;
    if (l == 10) p = woA + (size_t)e * 512 * 1024;
    if (l == 11) p = woA + (size_t)e * 512 * 1024 + 512 * 1024 - 4;
    if (l == 12) p = woB + (size_t)e * 512 * 1024;
    if (l == 13) p = woB + (size_t)e * 512 * 1024 + 512 * 1024 - 4;
    if (l == 14) p = ws + WS_O + (size_t)(b * 32 + 4 * e) * OFRAG_HEAD;
    if (l == 15) p = ws + WS_O + (size_t)((8 + b) * 32 + 4 * e) * OFRAG_HEAD;
    if (l == 16) p = slab_group(jb, 0);
    if (l == 17) p = slab_group(jb, 0) + 8 * SLAB_BYTES - 4;
    if (l == 18) p = slab_group(jb, 1);
    if (l == 19) p = slab_group(jb, 1) + 8 * SLAB_BYTES - 4;
    if (l == 20) p = out + (size_t)b * 144 * 2048;
    if (l == 21) p = out + (size_t)b * 144 * 2048 + 144 * 2048 - 4;
    if (l == 22) p = ws + WS_FLAG_O;
    set_m0(c.lds + LDS_NUL);
    asm volatile("global_load_lds_dword %0, off" ::"v"(p) : "memory");
  }
  if (c.w == 1) {
    u64 pc; int len;
    asm volatile("s_getpc_b64 %0\n.Lattn_code_from:\n\ts_mov_b32 %1, .Lattn_code_to - .Lattn_code_from" : "=s"(pc), "=s"(len));
    const rsrc_t code = mk_rsrc((const char*)pc, len > 0 ? len : 0);
    set_m0(c.lds + LDS_V);
    const unsigned lo = lane_id() * 16, so = r * 2048;
    DMA("", 0, lo, code, so); DMA("", 1024, lo, code, so);
  }
  const int c0 = (2 * b + 4 * (r & 3)) & 15;
  {
    if (c.w < 4) {
      Blk acc;
      first_projection_compute(c, acc);
      LSTAMP(1)
      *lds_ptr<bf16x8>(c.lds + qx_region(2) + 2 * 4096 + c.w * 1024 + lane_id() * 16) = pack_frag(acc.n[0] * QSCALE, acc.n[1] * QSCALE);
      bf16x8 Q[2][4];
#pragma unroll
      for (int q = 0; q < 2; ++q)
#pragma unroll
        for (int s = 0; s < 4; ++s) Q[q][s] = pack_frag(acc.a[q][2 * s] * QSCALE, acc.a[q][2 * s + 1] * QSCALE);
      lds_bar();
      jb.tag = epoch_tag(c);
      two_tile_wave(c, jb, Q);
      LSTAMP(9)
      out_projection_compute(c, jb);
    } else {
      first_projection_load(c, st, c0);
      LSTAMP(1)
      wait_bar<17>();
      jb.tag = epoch_tag(c);
      f32x4 accA[3][8];
      if (c.w == 4) {
        ninth_tile_wave(c, jb);
        LSTAMP(9)
        const Ldr T = make_ldr(0, 1024, true);
        tail_weights(c, jb, T, woB, 0, 4);
        wait_bar<6>();
#pragma clang loop unroll(disable)
        for (int i = 4; i < 7; ++i) { wait_bar<4>(); tail_weights(c, jb, T, woB, i, i + 1); }
      } else {
        projection_wave(c, st, c0, jb, woA, woB, accA);
      }
      out_projection_load(c, jb, woB, accA);
    }
  }
  LSTAMP(10)
  asm volatile(".Lattn_code_to:");
  reduce_out(c, jb, out);
}

extern "C" void kernel_launch(void* const* d_in, const int* in_sizes, int n_in, void* d_out, int out_size, void* d_ws, size_t ws_size,
                              hipStream_t stream) {
  (void)in_sizes; (void)n_in; (void)out_size; (void)ws_size;
  (void)hipFuncSetAttribute((const void*)attn_kernel, hipFuncAttributeMaxDynamicSharedMemorySize, LDS_TOTAL);
  hipLaunchKernelGGL(attn_kernel, dim3(256), dim3(512), LDS_TOTAL, stream, (const char*)d_in[0], (const char*)d_in[1], (const char*)d_in[2],
                     (const char*)d_in[3], (const char*)d_in[4], (const char*)d_in[5], (const char*)d_in[6], (const char*)d_in[7],
                     (const char*)d_in[8], (const char*)d_in[9], (char*)d_out, (char*)d_ws);
}
```
